```python
import torch

def _softmax(x, axis=-1):
    return torch.softmax(x, dim=axis)
def _gelu(x):
    return torch.nn.functional.gelu(x, approximate="tanh")
def _silu(x):
    return torch.nn.functional.silu(x)
def _sigmoid(x):
    return torch.sigmoid(x)
def _layernorm(x, axis=-1, eps=1e-5):
    m = x.mean(dim=axis, keepdim=True)
    v = x.var(dim=axis, keepdim=True, unbiased=False)
    return (x - m) / torch.sqrt(v + eps)
def _scatter_add(tgt, idx, src, axis=0):
    return tgt.clone().index_add_(axis, idx, src)
def _segment_sum(src, seg, n):
    out = torch.zeros((n,) + src.shape[1:], dtype=src.dtype)
    return out.index_add_(0, seg, src)
def _pad(x, pw):
    flat = tuple(v for pair in reversed(pw) for v in pair)
    return torch.nn.functional.pad(x, flat)

kernel_name = 'recurrent_scan__gqa_block_cached__dense_activation_squared_relu__decode'

def setup_inputs():

    _w = (lambda a: (lambda t: t.to(torch.bfloat16) if t.is_floating_point() else t)(torch.as_tensor(a)))
    def _A_setup():
        B, Sq, D = 4, 1, 512
        Hq, Hkv, Sk, Dh = 32, 8, 16384, 128
        assert Hq % Hkv == 0, (Hq, Hkv)
        return {
            "x": _w((lambda *s: torch.randn(*s, dtype=torch.bfloat16) * 0.05)(B, Sq, D)),
            "k_cache": _w((lambda *s: torch.randn(*s, dtype=torch.bfloat16) * 1.6)(B, Hkv, Sk, Dh)),
            "v_cache": _w((lambda *s: torch.randn(*s, dtype=torch.bfloat16) * 0.2)(B, Hkv, Sk, Dh)),
            "wq": _w((lambda *s: torch.randn(*s, dtype=torch.bfloat16) * 1.6)(D, Hq * Dh) * (D ** -0.5)),
            "wo": _w((lambda *s: torch.randn(*s, dtype=torch.bfloat16) * 0.2)(Hq * Dh, D) * ((Hq * Dh) ** -0.5)),
        }
    _A_in = _A_setup()

    def _B_setup():
        B, S, D, F = 4, 1, 512, 1792
        return {
            "x": _w((lambda *s: torch.randn(*s, dtype=torch.bfloat16) * 0.05)(B, S, D)),
            "w1": _w((lambda *s: torch.randn(*s, dtype=torch.bfloat16) * 0.1)(D, F)),
            "b1": _w((lambda *s: torch.randn(*s, dtype=torch.bfloat16) * 0.1)(F)),
            "w2": _w((lambda *s: torch.randn(*s, dtype=torch.bfloat16) * 0.1)(F, D)),
            "b2": _w((lambda *s: torch.randn(*s, dtype=torch.bfloat16) * 0.1)(D)),
        }
    _B_in = _B_setup()

    return {
        "A_x": _A_in["x"],
        "A_k_cache": _A_in["k_cache"],
        "A_v_cache": _A_in["v_cache"],
        "A_wq": _A_in["wq"],
        "A_wo": _A_in["wo"],
        "B_x": _B_in["x"],
        "B_w1": _B_in["w1"],
        "B_b1": _B_in["b1"],
        "B_w2": _B_in["w2"],
        "B_b2": _B_in["b2"],
    }

def reference(A_x, A_k_cache, A_v_cache, A_wq, A_wo, B_x, B_w1, B_b1, B_w2, B_b2):
    def _A_ref(x, k_cache, v_cache, wq, wo):
        B, Sq, D = x.shape
        Hq, Dh = 32, 128
        Hkv = k_cache.shape[1]
        rep = Hq // Hkv
        q = (lambda x, *d: x.permute(*d))((x @ wq).reshape(B, Sq, Hq, Dh), 0, 2, 1, 3)
        k = (lambda x, n, axis: torch.repeat_interleave(x, n, dim=axis))(k_cache, rep, axis=1)
        v = (lambda x, n, axis: torch.repeat_interleave(x, n, dim=axis))(v_cache, rep, axis=1)
        scores = torch.einsum("bhqd,bhkd->bhqk", q, k) * (Dh ** -0.5)
        p = _softmax(scores, axis=-1)
        o = torch.einsum("bhqk,bhkd->bhqd", p, v)
        return (lambda x, *d: x.permute(*d))(o, 0, 2, 1, 3).reshape(B, Sq, Hq * Dh) @ wo

    def _B_ref(x, w1, b1, w2, b2):
        h = torch.einsum("bsd,df->bsf", x, w1) + b1
        zero = torch.zeros_like(h)
        r = torch.where(h > zero, h, zero)
        h = r * r
        out = torch.einsum("bsf,fd->bsd", h, w2) + b2
        return out

    x = A_x
    xn = _layernorm(x, axis=-1)
    s = _A_ref(xn, A_k_cache, A_v_cache, A_wq, A_wo)
    g = _silu(_B_ref(xn, B_w1, B_b1, B_w2, B_b2))
    return x + s * g
```

```cpp
#include <hip/hip_runtime.h>
#include <hip/hip_bf16.h>
#include <stdint.h>

typedef unsigned short u16;
static __device__ __forceinline__ float bf2f(u16 v) { return __uint_as_float(((uint32_t)v) << 16); }
static __device__ __forceinline__ u16 f2bf(float f) {
    uint32_t u = __float_as_uint(f);
    if ((u & 0x7fffffffu) > 0x7f800000u) return (u16)((u >> 16) | 0x40);
    u += 0x7fffu + ((u >> 16) & 1u);
    return (u16)(u >> 16);
}
static __device__ __forceinline__ float rbf(float f) { return bf2f(f2bf(f)); }

#define NB 4
#define DM 512
#define HQ 32
#define HKV 8
#define SK 16384
#define DH 128
#define FF 1792
#define NSPLIT 64
#define TOK (SK / NSPLIT)

__global__ void k_ln(const u16* x, float* xn) {
    int b = blockIdx.x; int t = threadIdx.x;
    __shared__ float red[512];
    float xv = bf2f(x[b * DM + t]);
    red[t] = xv; __syncthreads();
    for (int s = 256; s > 0; s >>= 1) { if (t < s) red[t] += red[t + s]; __syncthreads(); }
    float mean = red[0] / DM; __syncthreads();
    float d = xv - mean;
    red[t] = d * d; __syncthreads();
    for (int s = 256; s > 0; s >>= 1) { if (t < s) red[t] += red[t + s]; __syncthreads(); }
    float var = red[0] / DM;
    float m = rbf(mean), v = rbf(var);
    float num = rbf(xv - m);
    float den = rbf(sqrtf(rbf(v + 1e-5f)));
    xn[b * DM + t] = rbf(num / den);
}
__global__ void k_qh(const float* xn, const u16* wq, const u16* w1, const u16* b1, float* q, float* h) {
    int c = blockIdx.x * 256 + threadIdx.x;
    int b = blockIdx.y;
    if (c < 4096) {
        float a = 0.f;
        for (int k = 0; k < DM; ++k) a += xn[b * DM + k] * bf2f(wq[k * 4096 + c]);
        q[b * 4096 + c] = rbf(a);
    } else if (c < 4096 + FF) {
        int f = c - 4096;
        float a = 0.f;
        for (int k = 0; k < DM; ++k) a += xn[b * DM + k] * bf2f(w1[k * FF + f]);
        float hh = rbf(rbf(a) + bf2f(b1[f]));
        float r = hh > 0.f ? hh : 0.f;
        h[b * FF + f] = rbf(r * r);
    }
}
__global__ void k_attn(const float* q, const u16* kc, const u16* vc, float* pm, float* pl, float* pO) {
    int split = blockIdx.x, hkv = blockIdx.y, b = blockIdx.z; int t = threadIdx.x;
    __shared__ float qs[4][128];
    __shared__ float sc[4][256];
    __shared__ float red[256];
    __shared__ float mx[4], sm[4];
    for (int i = t; i < 512; i += 256) qs[i >> 7][i & 127] = q[b * 4096 + hkv * 512 + i];
    __syncthreads();
    const u16* kp = kc + ((size_t)(b * HKV + hkv) * SK + split * TOK + t) * DH;
    float a[4] = {0, 0, 0, 0};
    for (int d = 0; d < DH; ++d) { float kv = bf2f(kp[d]); for (int h = 0; h < 4; ++h) a[h] += qs[h][d] * kv; }
    for (int h = 0; h < 4; ++h) sc[h][t] = rbf(rbf(a[h]) * 0.08838834764831845f);
    __syncthreads();
    for (int h = 0; h < 4; ++h) {
        red[t] = sc[h][t]; __syncthreads();
        for (int s = 128; s > 0; s >>= 1) { if (t < s) red[t] = fmaxf(red[t], red[t + s]); __syncthreads(); }
        if (t == 0) mx[h] = red[0];
        __syncthreads();
        float p = expf(sc[h][t] - mx[h]);
        sc[h][t] = p;
        red[t] = p; __syncthreads();
        for (int s = 128; s > 0; s >>= 1) { if (t < s) red[t] += red[t + s]; __syncthreads(); }
        if (t == 0) sm[h] = red[0];
        __syncthreads();
    }
    const u16* vp = vc + ((size_t)(b * HKV + hkv) * SK + split * TOK) * DH;
    for (int i = t; i < 512; i += 256) {
        int h = i >> 7, d = i & 127;
        float o = 0.f;
        for (int k = 0; k < TOK; ++k) o += rbf(sc[h][k]) * bf2f(vp[k * DH + d]);
        pO[((size_t)(b * HQ + hkv * 4 + h) * NSPLIT + split) * DH + d] = o;
    }
    if (t < 4) { pm[(b * HQ + hkv * 4 + t) * NSPLIT + split] = mx[t]; pl[(b * HQ + hkv * 4 + t) * NSPLIT + split] = sm[t]; }
}
__global__ void k_merge(const float* pm, const float* pl, const float* pO, float* o) {
    int bh = blockIdx.x; int d = threadIdx.x;
    float M = -INFINITY;
    for (int s = 0; s < NSPLIT; ++s) M = fmaxf(M, pm[bh * NSPLIT + s]);
    float L = 0.f, O = 0.f;
    for (int s = 0; s < NSPLIT; ++s) { float w = expf(pm[bh * NSPLIT + s] - M); L += w * pl[bh * NSPLIT + s]; O += w * pO[((size_t)bh * NSPLIT + s) * DH + d]; }
    o[bh * DH + d] = rbf(O / L);
}
__global__ void k_fin(const u16* x, const float* o, const float* h, const u16* wo, const u16* w2, const u16* b2, u16* out) {
    int b = blockIdx.y; int c = blockIdx.x * 64 + threadIdx.x;
    float s = 0.f;
    for (int k = 0; k < 4096; ++k) s += o[b * 4096 + k] * bf2f(wo[k * DM + c]);
    s = rbf(s);
    float g = 0.f;
    for (int k = 0; k < FF; ++k) g += h[b * FF + k] * bf2f(w2[k * DM + c]);
    g = rbf(rbf(g) + bf2f(b2[c]));
    g = rbf(g / (1.f + expf(-g)));
    out[b * DM + c] = f2bf(bf2f(x[b * DM + c]) + rbf(s * g));
}

extern "C" void kernel_launch(void* const* d_in, const int* in_sizes, int n_in,
                              void* d_out, int out_size, void* d_ws, size_t ws_size,
                              hipStream_t stream) {
    const u16* x = (const u16*)d_in[0];
    const u16* kc = (const u16*)d_in[1];
    const u16* vc = (const u16*)d_in[2];
    const u16* wq = (const u16*)d_in[3];
    const u16* wo = (const u16*)d_in[4];
    const u16* w1 = (const u16*)d_in[6];
    const u16* b1 = (const u16*)d_in[7];
    const u16* w2 = (const u16*)d_in[8];
    const u16* b2 = (const u16*)d_in[9];
    float* ws = (float*)d_ws;
    float* xn = ws; float* q = xn + 2048; float* h = q + 16384; float* o = h + 4 * FF + 0;
    float* pm = o + 16384; float* pl = pm + 128 * NSPLIT; float* pO = pl + 128 * NSPLIT;
    k_ln<<<4, 512, 0, stream>>>(x, xn);
    k_qh<<<dim3(23, 4), 256, 0, stream>>>(xn, wq, w1, b1, q, h);
    k_attn<<<dim3(NSPLIT, HKV, NB), 256, 0, stream>>>(q, kc, vc, pm, pl, pO);
    k_merge<<<128, 128, 0, stream>>>(pm, pl, pO, o);
    k_fin<<<dim3(8, 4), 64, 0, stream>>>(x, o, h, wo, w2, b2, (u16*)d_out);
}
```

```cpp
#include <hip/hip_runtime.h>
#include <stdint.h>

typedef uint32_t u32;
typedef uint32_t u32x2 __attribute__((ext_vector_type(2)));
typedef uint32_t u32x4 __attribute__((ext_vector_type(4)));
typedef float f32x4 __attribute__((ext_vector_type(4)));
typedef __bf16 bf16x8 __attribute__((ext_vector_type(8)));
typedef __bf16 bf16x2 __attribute__((ext_vector_type(2)));
typedef __amdgpu_buffer_rsrc_t rsrc_t;

#define RING 32
#define SPIN_MAX (1 << 15)

#define WS_EPOCH 0
#define WS_QX 4096
#define WS_H1 (WS_QX + 65536)
#define WS_G2 (WS_H1 + 28 * 8 * 2048)
#define WS_ML (WS_G2 + 256 * 2048)
#define WS_OA (WS_ML + 32 * 8 * 64)
#define WS_SB (WS_OA + 256 * 4096)
#define WS_END (WS_SB + 256 * 4096)
#define WS_STAMP (8u << 20)

enum { C_XN, C_QP, F_Q, C_P1, C_H, C_P2, C_ML, F_AL, C_O, F_SD, C_N = 16 };

struct __attribute__((aligned(16))) Lds {
    u32 cnt[C_N];
    u32 xnb[4][256];
    float qpart[4][4][16];
    u32 qs[256];
    float part1[4][4][64];
    float part2[4][4][64];
    u32 hb[4][32];
    float ml[4][4][2];
    float alpha[4][4];
    float ored[4][512];
    u32 tag[4];
};

typedef __attribute__((address_space(3))) void* ldsp;
static __device__ __forceinline__ float asf(u32 v) { return __uint_as_float(v); }
static __device__ __forceinline__ u32 asu(float v) { return __float_as_uint(v); }
static __device__ __forceinline__ float blo(u32 v) { return asf(v << 16); }
static __device__ __forceinline__ float bhi(u32 v) { return asf(v & 0xffff0000u); }
static __device__ __forceinline__ u32 pk(float lo, float hi) {
    bf16x2 r = {(__bf16)lo, (__bf16)hi};
    return __builtin_bit_cast(u32, r);
}
static __device__ __forceinline__ float rbf(float v) { return blo(pk(v, v)); }
template <int CTRL> static __device__ __forceinline__ float dpp(float v) {
    return asf(__builtin_amdgcn_update_dpp(0u, asu(v), CTRL, 0xf, 0xf, true));
}
#define ROR8 0x128
#define ROR4 0x124
#define ROR2 0x122
#define ROR1 0x121
static __device__ __forceinline__ float sw32(float a, float b) {
    auto r = __builtin_amdgcn_permlane32_swap(asu(a), asu(b), false, false);
    return asf(r[0]) + asf(r[1]);
}
static __device__ __forceinline__ float sw16(float a, float b) {
    auto r = __builtin_amdgcn_permlane16_swap(asu(a), asu(b), false, false);
    return asf(r[0]) + asf(r[1]);
}
static __device__ __forceinline__ float mx32(float a) {
    auto r = __builtin_amdgcn_permlane32_swap(asu(a), asu(a), false, false);
    return fmaxf(asf(r[0]), asf(r[1]));
}
static __device__ __forceinline__ float mx16(float a) {
    auto r = __builtin_amdgcn_permlane16_swap(asu(a), asu(a), false, false);
    return fmaxf(asf(r[0]), asf(r[1]));
}
static __device__ __forceinline__ float dot2(u32 a, u32 b, float c) {
    return __builtin_amdgcn_fdot2_f32_bf16(__builtin_bit_cast(bf16x2, a), __builtin_bit_cast(bf16x2, b), c, false);
}
static __device__ __forceinline__ u32 prm(u32 hi, u32 lo, u32 sel) { return __builtin_amdgcn_perm(hi, lo, sel); }
#define SEL_LO 0x05040100u
#define SEL_HI 0x07060302u
static __device__ __forceinline__ f32x4 mfma(u32x4 a, u32x4 b, f32x4 c) {
    return __builtin_amdgcn_mfma_f32_16x16x32_bf16(__builtin_bit_cast(bf16x8, a), __builtin_bit_cast(bf16x8, b), c, 0, 0, 0);
}
static __device__ __forceinline__ rsrc_t mkrs(const void* p, u32 bytes) {
    return __builtin_amdgcn_make_buffer_rsrc((void*)p, 0, bytes, 0x00020000);
}
static __device__ __forceinline__ u32x4 ld16(rsrc_t r, int vo, int so) { return __builtin_amdgcn_raw_buffer_load_b128(r, vo, so, 0); }
#define AUXS 2
#define AUXN "nt"
static __device__ __forceinline__ u32x4 ldnt(rsrc_t r, int vo, int so) { return __builtin_amdgcn_raw_buffer_load_b128(r, vo, so, AUXS); }
#define AUXW 0
#define AUXO 0
#define AUXL 16
#define AUXP 16
static __device__ __forceinline__ u32x4 ldw(rsrc_t r, int vo, int so) { return __builtin_amdgcn_raw_buffer_load_b128(r, vo, so, AUXW); }
static __device__ __forceinline__ u32x4 ldo(rsrc_t r, int vo, int so) { return __builtin_amdgcn_raw_buffer_load_b128(r, vo, so, AUXO); }
static __device__ __forceinline__ u32x4 ldsc(rsrc_t r, int vo) { return __builtin_amdgcn_raw_buffer_load_b128(r, vo, 0, AUXL); }
static __device__ __forceinline__ void gput(rsrc_t r, int vo, u32 v0, u32 v1, u32 tA, u32 tB) {
    u32x4 g = {v0, tA, v1, tB};
    __builtin_amdgcn_raw_buffer_store_b128(g, r, vo, 0, AUXP);
}
#define LGKM0() asm volatile("s_waitcnt lgkmcnt(0)" ::: "memory")
#define CFENCE() asm volatile("" ::: "memory")
static __device__ __forceinline__ void larrive(u32* c) {
    LGKM0();
    if ((threadIdx.x & 63) == 0) __hip_atomic_fetch_add(c, 1u, __ATOMIC_RELAXED, __HIP_MEMORY_SCOPE_WORKGROUP);
    CFENCE();
}
template <int SLEEP = 1>
static __device__ __forceinline__ void lwait(u32* c, u32 target) {
#pragma unroll 1
    for (int it = 0; it < SPIN_MAX; ++it) {
        u32 v = __builtin_amdgcn_readfirstlane(__hip_atomic_load(c, __ATOMIC_RELAXED, __HIP_MEMORY_SCOPE_WORKGROUP));
        if (v >= target) break;
        __builtin_amdgcn_s_sleep(SLEEP);
    }
    CFENCE();
}
#pragma clang diagnostic ignored "-Winline-asm"
static __device__ __forceinline__ void dma16(u32 ldsaddr, int vo, u32x4 rs, int so) {
    asm volatile("s_mov_b32 m0, %0\n\ts_nop 0\n\tbuffer_load_dwordx4 %1, %2, %3 offen " AUXN " lds" ::"s"(ldsaddr), "v"(vo), "s"(rs), "s"(so) : "m0");
}
static __device__ __forceinline__ u32 now16() { return (u32)__builtin_amdgcn_s_memrealtime() << 4; }
template <int SLEEP = 1>
static __device__ __forceinline__ void until(u32 t) {
#pragma unroll 1
    for (int it = 0; it < 4096; ++it) {
        if ((int)(now16() - t) >= 0) break;
        __builtin_amdgcn_s_sleep(SLEEP);
    }
}
static __device__ __forceinline__ void pace(u32& tn, u32 step) {
    until<1>(tn);
    tn += step;
}
#define PACE 880
#define US16(x) ((u32)((x) * 1600))
#define PRE 0
#define PA PACE
#define PB PA
#define PC PB
#define PD PACE
#define PE PACE
#define QPOLL 4.0
#define HPOLL 9.0
#define WPOLL 12.0
template <int N, int SLEEP>
static __device__ __forceinline__ void gsweep(u32x4 (&g)[N], rsrc_t r, const int (&vo)[N], u32 tA, u32 tB) {
#pragma unroll 1
    for (int it = 0; it < SPIN_MAX / 8; ++it) {
        CFENCE();
        bool ok = true;
#pragma unroll
        for (int k = 0; k < N; ++k) g[k] = ldsc(r, vo[k]);
#pragma unroll
        for (int k = 0; k < N; ++k) ok = ok && g[k][1] == tA && g[k][3] == tB;
        if (__all(ok)) break;
        __builtin_amdgcn_s_sleep(SLEEP);
    }
    CFENCE();
}
static __device__ __forceinline__ void tile_mac(u32x4 wa, u32x4 wb, const u32 (&a)[4], float (&o)[8]) {
    float acc[4][8];
#pragma unroll
    for (int e = 0; e < 8; ++e) {
        u32 wp = prm(wb[e >> 1], wa[e >> 1], (e & 1) ? SEL_HI : SEL_LO);
#pragma unroll
        for (int b = 0; b < 4; ++b) acc[b][e] = dot2(wp, a[b], 0.f);
    }
#pragma unroll
    for (int e = 0; e < 8; ++e) {
        float r0 = sw32(acc[0][e], acc[2][e]), r1 = sw32(acc[1][e], acc[3][e]);
        float r = sw16(r0, r1);
        o[e] = r + dpp<ROR8>(r);
    }
}

#define STAMP(i) do { } while (0)
#define STAMPDUMP() do { } while (0)
#define VMW(n) asm volatile("s_waitcnt vmcnt(" #n ")" ::: "memory")

__global__ __launch_bounds__(512, 2) void fused_decode(
    const uint16_t* __restrict__ x, const uint16_t* __restrict__ kc, const uint16_t* __restrict__ vc,
    const uint16_t* __restrict__ wq, const uint16_t* __restrict__ wo, const uint16_t* __restrict__ w1,
    const uint16_t* __restrict__ b1, const uint16_t* __restrict__ w2, const uint16_t* __restrict__ b2,
    uint16_t* __restrict__ out, char* __restrict__ ws) {
    __shared__ Lds L;
    __shared__ u32x4 KB[4][32][64];
    const int lane = threadIdx.x & 63;
    const int wave = __builtin_amdgcn_readfirstlane(threadIdx.x >> 6);
    const int blk = blockIdx.x;
    const int hkv = blk & 7, j = blk >> 3, b = j >> 3, sp = j & 7;
    const int grp = b * 8 + hkv;
    const int r16 = lane & 15, g4 = lane >> 4;
    if (wave == 0) {
        if (lane < C_N) L.cnt[lane] = 0;
        LGKM0();
    }
    __builtin_amdgcn_s_barrier();
    asm volatile("" ::"s"(x), "s"(kc), "s"(vc), "s"(wq), "s"(wo), "s"(w1), "s"(b1), "s"(w2), "s"(b2), "s"(out), "s"(ws));
    STAMP(16);
    const u32 t0 = now16();
    const rsrc_t rws = mkrs(ws, WS_END);

    if (wave >= 4) {
        const int w = wave - 4;
        const size_t kvoff = ((size_t)grp * 16384 + sp * 2048) * 128;
        const rsrc_t rk = mkrs(kc + kvoff, 2048 * 256);
        const int tau = r16 >> 1;
        const int vok = w * 4096 + (4 * (tau & 1) + (tau >> 1)) * 256 + (r16 & 1) * 64 + g4 * 16;
        const int vov = w * 4096 + lane * 16;
#define KO1 128
#define KO2 2048
#define KO3 2176
        u32x4 ring[32];
        u32 tn = t0 - PRE * PACE;
#pragma unroll
        for (int n = 0; n < 32; ++n) {
            if ((n & 3) == 0) { __builtin_amdgcn_sched_barrier(0); pace(tn, PA); __builtin_amdgcn_sched_barrier(0); }
            ring[n] = ldnt(rk, vok + ((n & 3) == 0 ? 0 : (n & 3) == 1 ? KO1 : (n & 3) == 2 ? KO2 : KO3), (n >> 2) * 16384);
        }
        __builtin_amdgcn_sched_barrier(0);
        STAMP(17);
        const u32 kb = (u32)(uintptr_t)(ldsp)&KB[w][0][0];
        {
            const uint64_t pa = (uint64_t)(kc + kvoff);
            const u32x4 rs = {(u32)pa, (u32)(pa >> 32) & 0xffffu, 2048u * 256u, 0x00020000u};
#pragma unroll 1
            for (int i = 0; i < 8; ++i) {
                pace(tn, PB);
                const u32 la = kb + i * 4096;
                const int so = (8 + i) * 16384;
                dma16(la, vok, rs, so);
                dma16(la + 1024, vok + KO1, rs, so);
                dma16(la + 2048, vok + KO2, rs, so);
                dma16(la + 3072, vok + KO3, rs, so);
            }
        }
        __builtin_amdgcn_sched_barrier(0);
        STAMP(5);
        lwait(&L.cnt[F_Q], 1);
        STAMP(1);
        { u32 t = now16(); if ((int)(t - tn) > 0) tn = t; }
        u32x4 F[2][2];
        {
            const int sq = r16 >> 3, uq = (r16 >> 2) & 1, h = r16 & 3;
#pragma unroll
            for (int half = 0; half < 2; ++half) {
                u32x4 q = *(const u32x4*)&L.qs[(h * 128 + 64 * half + 32 * uq + 8 * g4) / 2];
#pragma unroll
                for (int s = 0; s < 2; ++s) {
                    u32x4 z = {0, 0, 0, 0};
                    F[s][half] = (sq == s) ? q : z;
                }
            }
        }
        const bool uq1 = (r16 & 4) != 0;
        u32 R[12], T[4];
#pragma unroll
        for (int k = 0; k < 12; ++k) R[k] = 0;
#pragma unroll
        for (int k = 0; k < 4; ++k) T[k] = 0;
        float mrun = -INFINITY;
#define SCORE(a0, a1, a2, a3, sc) do { \
            f32x4 d = {0.f, 0.f, 0.f, 0.f}; \
            d = mfma(a0, F[0][0], d); d = mfma(a1, F[0][1], d); d = mfma(a2, F[1][0], d); d = mfma(a3, F[1][1], d); \
            asm volatile("" : "+v"(d)); \
            dd = d; } while (0)
#define SCFIN(sc) do { \
            u32 t_ = __builtin_amdgcn_update_dpp(0u, asu(dd[1]), 0x104, 0xf, 0x5, false); \
            t_ = __builtin_amdgcn_update_dpp(t_, asu(dd[2]), 0x114, 0xf, 0xa, false); \
            sc = (uq1 ? dd[3] : dd[0]) + asf(t_); } while (0)
#define SCPK(dst, sa, sb) do { \
            u32 p1 = pk(sa, sb); \
            u32 p2 = pk(blo(p1) * 0.08838834764831845f, bhi(p1) * 0.08838834764831845f); \
            dst = p2; \
            mrun = fmaxf(mrun, fmaxf(blo(p2), bhi(p2))); } while (0)
        {
            const uint16_t* pb = kc + kvoff;
            int vo0 = vok, vo1 = vok + KO1, vo2 = vok + KO2, vo3 = vok + KO3, so = 16 * 16384;
#pragma unroll 1
            for (int it = 0; it < 3; ++it) {
                if (it == 2) { pb = vc + kvoff; vo0 = vov; vo1 = vov + 1024; vo2 = vov + 2048; vo3 = vov + 3072; so = 0; }
                const rsrc_t rn = mkrs(pb, 2048 * 256);
                const u32 pstep = it == 0 ? PC : it == 1 ? PD : PE;
                float sprev = 0.f, lprev = 0.f;
#pragma unroll
                for (int k = 0; k < 8; ++k) R[k] = R[k + 4];
#pragma unroll
                for (int i = 0; i < 8; ++i) {
                    f32x4 dd;
                    SCORE(ring[4 * i], ring[4 * i + 1], ring[4 * i + 2], ring[4 * i + 3], sc);
                    __builtin_amdgcn_sched_barrier(0);
                    pace(tn, pstep);
                    __builtin_amdgcn_sched_barrier(0);
                    ring[4 * i + 0] = ldnt(rn, vo0, so + i * 16384);
                    ring[4 * i + 1] = ldnt(rn, vo1, so + i * 16384);
                    ring[4 * i + 2] = ldnt(rn, vo2, so + i * 16384);
                    ring[4 * i + 3] = ldnt(rn, vo3, so + i * 16384);
                    __builtin_amdgcn_sched_barrier(0);
                    float sc;
                    SCFIN(sc);
                    if (i & 1) SCPK(R[8 + (i >> 1)], sprev, sc); else sprev = sc;
                    if (it == 1) {
                        u32x4 a0, a1, a2, a3;
                        asm volatile("ds_read_b128 %0, %4 offset:%5\n\tds_read_b128 %1, %4 offset:%6\n\tds_read_b128 %2, %4 offset:%7\n\t"
                                     "ds_read_b128 %3, %4 offset:%8\n\ts_waitcnt lgkmcnt(0)"
                                     : "=&v"(a0), "=&v"(a1), "=&v"(a2), "=&v"(a3)
                                     : "v"(kb + lane * 16), "n"(i * 4096), "n"(i * 4096 + 1024), "n"(i * 4096 + 2048), "n"(i * 4096 + 3072), "v"(dd[0]));
                        SCORE(a0, a1, a2, a3, lc);
                        float lc;
                        SCFIN(lc);
                        if (i & 1) SCPK(T[i >> 1], lprev, lc); else lprev = lc;
                    }
                    __builtin_amdgcn_sched_barrier(0);
                }
                so += 8 * 16384;
                STAMP(8 + it);
            }
        }
        STAMP(2);
        u32 S[16];
#pragma unroll
        for (int k = 0; k < 4; ++k) { S[k] = R[k]; S[4 + k] = T[k]; S[8 + k] = R[4 + k]; S[12 + k] = R[8 + k]; }
        mrun = fmaxf(mrun, dpp<ROR4>(mrun));
        mrun = fmaxf(mrun, dpp<ROR8>(mrun));
        mrun = mx32(mx16(mrun));
        float lsum = 0.f;
#pragma unroll
        for (int k = 0; k < 16; ++k) {
            float p0 = __builtin_amdgcn_exp2f((blo(S[k]) - mrun) * 1.4426950408889634f);
            float p1 = __builtin_amdgcn_exp2f((bhi(S[k]) - mrun) * 1.4426950408889634f);
            lsum += p0 + p1;
            S[k] = pk(p0, p1);
        }
        lsum += dpp<ROR4>(lsum);
        lsum += dpp<ROR8>(lsum);
        lsum = sw16(lsum, lsum);
        lsum = sw32(lsum, lsum);
        if (lane < 4) { L.ml[w][lane][0] = mrun; L.ml[w][lane][1] = lsum; }
        larrive(&L.cnt[C_ML]);
        u32 sel[4];
        {
            const int q4 = r16 >> 2;
            const u32 s0 = (q4 & 1) ? 0x01000c0cu : 0x0c0c0100u, s1 = (q4 & 1) ? 0x03020c0cu : 0x0c0c0302u;
            sel[0] = (q4 < 2) ? s0 : 0x0c0c0c0cu; sel[1] = (q4 >= 2) ? s0 : 0x0c0c0c0cu;
            sel[2] = (q4 < 2) ? s1 : 0x0c0c0c0cu; sel[3] = (q4 >= 2) ? s1 : 0x0c0c0c0cu;
        }
        f32x4 acc[8];
#pragma unroll
        for (int e = 0; e < 8; ++e) acc[e] = (f32x4){0.f, 0.f, 0.f, 0.f};
        {
            int so = 8 * 16384;
#pragma unroll 1
            for (int it = 0; it < 4; ++it) {
                const rsrc_t rn = mkrs(vc + kvoff, it < 3 ? 2048 * 256 : 0);
#pragma unroll
                for (int t = 0; t < 4; ++t) {
                    u32x4 A = {prm(S[t], S[t], sel[0]), prm(S[t], S[t], sel[1]), prm(S[t], S[t], sel[2]), prm(S[t], S[t], sel[3])};
#pragma unroll
                    for (int e = 0; e < 8; ++e) {
                        u32x4 B;
#pragma unroll
                        for (int p = 0; p < 4; ++p)
                            B[p] = prm(ring[8 * t + 2 * p + 1][e >> 1], ring[8 * t + 2 * p][e >> 1], (e & 1) ? SEL_HI : SEL_LO);
                        acc[e] = mfma(A, B, acc[e]);
                    }
                    asm volatile("" : "+v"(acc[0]), "+v"(acc[1]), "+v"(acc[2]), "+v"(acc[3]), "+v"(acc[4]), "+v"(acc[5]), "+v"(acc[6]), "+v"(acc[7]));
                    __builtin_amdgcn_sched_barrier(0);
                    if (it < 3) pace(tn, PACE);
                    __builtin_amdgcn_sched_barrier(0);
#pragma unroll
                    for (int k = 0; k < 4; ++k) ring[8 * t + k] = ldnt(rn, vov + k * 1024, so + (2 * t) * 16384);
                    __builtin_amdgcn_sched_barrier(0);
                    if (it < 3) pace(tn, PACE);
                    __builtin_amdgcn_sched_barrier(0);
#pragma unroll
                    for (int k = 4; k < 8; ++k) ring[8 * t + k] = ldnt(rn, vov + (k & 3) * 1024, so + (2 * t + 1) * 16384);
                    __builtin_amdgcn_sched_barrier(0);
                }
#pragma unroll
                for (int k = 0; k < 12; ++k) S[k] = S[k + 4];
                so += 8 * 16384;
                STAMP(12 + it);
            }
        }
        STAMP(3);
        lwait(&L.cnt[F_AL], 1);
        const float al = L.alpha[w][g4];
        f32x4 o0, o1;
#pragma unroll
        for (int e = 0; e < 8; ++e) {
            float v = sw16(sw32(acc[e][0], acc[e][2]), sw32(acc[e][1], acc[e][3])) * al;
            if (e < 4) o0[e] = v; else o1[e - 4] = v;
        }
        *(f32x4*)&L.ored[w][g4 * 128 + 8 * r16] = o0;
        *(f32x4*)&L.ored[w][g4 * 128 + 8 * r16 + 4] = o1;
        LGKM0();
        u32 ticket = 0;
        if (lane == 0) ticket = __hip_atomic_fetch_add(&L.cnt[C_O], 1u, __ATOMIC_RELAXED, __HIP_MEMORY_SCOPE_WORKGROUP);
        ticket = __builtin_amdgcn_readfirstlane(ticket);
        CFENCE();
        if (ticket == 3) {
            f32x4 s0 = *(f32x4*)&L.ored[0][g4 * 128 + 8 * r16], s1 = *(f32x4*)&L.ored[0][g4 * 128 + 8 * r16 + 4];
#pragma unroll
            for (int k = 1; k < 4; ++k) {
                s0 += *(f32x4*)&L.ored[k][g4 * 128 + 8 * r16];
                s1 += *(f32x4*)&L.ored[k][g4 * 128 + 8 * r16 + 4];
            }
            const u32 tA = L.tag[0], tB = L.tag[1];
            const int vo = WS_OA + ((grp * 8 + 2 * g4 + (r16 >> 3)) * 8 + sp) * 512 + (r16 & 7) * 16;
            gput(rws, vo, asu(s0[0]), asu(s0[1]), tA, tB);
            gput(rws, vo + 128, asu(s0[2]), asu(s0[3]), tA, tB);
            gput(rws, vo + 256, asu(s1[0]), asu(s1[1]), tA, tB);
            gput(rws, vo + 384, asu(s1[2]), asu(s1[3]), tA, tB);
            if (lane == 0) __hip_atomic_store(&L.cnt[F_SD], 1u, __ATOMIC_RELAXED, __HIP_MEMORY_SCOPE_WORKGROUP);
        }
        STAMP(4);
        STAMPDUMP();
        return;
    }

    const int hw = wave;
    const u32 epoch = __hip_atomic_load((const u32*)(ws + WS_EPOCH), __ATOMIC_RELAXED, __HIP_MEMORY_SCOPE_AGENT);
    const u32x4 xv = ld16(mkrs(x, 4096), hw * 1024 + lane * 16, 0);
    const rsrc_t rwq = mkrs(wq, 512 * 4096 * 2);
    u32x4 wqr[4];
    {
        const int vo = (hw * 128 + (lane >> 1) * 4) * 8192 + (hkv * 512 + j * 16 + (lane & 1) * 8) * 2;
#pragma unroll
        for (int i = 0; i < 4; ++i) wqr[i] = ldw(rwq, vo, i * 8192);
    }
    const bool mlp = blk < 224;
    const int fs = mlp ? (blk >> 3) : 0, ks = blk & 7;
    u32x4 w1a, w1b;
    {
        const rsrc_t r1 = mkrs(w1, 512 * 1792 * 2);
        const int vo = (ks * 64 + hw * 16 + (lane >> 3) * 2) * 3584 + (fs * 64 + (lane & 7) * 8) * 2;
        w1a = ldw(r1, vo, 0);
        w1b = ldw(r1, vo, 3584);
    }
    __builtin_amdgcn_sched_barrier(0);
    STAMP(17);
    const u32 tA = epoch + 1u, tB = tA ^ 0x5a5a5a5au;
    STAMP(11);
    {
        float v[8];
#pragma unroll
        for (int k = 0; k < 4; ++k) { v[2 * k] = blo(xv[k]); v[2 * k + 1] = bhi(xv[k]); }
        float s = ((v[0] + v[1]) + (v[2] + v[3])) + ((v[4] + v[5]) + (v[6] + v[7]));
        s += dpp<ROR1>(s); s += dpp<ROR2>(s); s += dpp<ROR4>(s); s += dpp<ROR8>(s);
        s = sw16(s, s); s = sw32(s, s);
        const float mean = s * (1.f / 512.f);
        float q = 0.f;
#pragma unroll
        for (int k = 0; k < 8; ++k) { float d = v[k] - mean; q = fmaf(d, d, q); }
        q += dpp<ROR1>(q); q += dpp<ROR2>(q); q += dpp<ROR4>(q); q += dpp<ROR8>(q);
        q = sw16(q, q); q = sw32(q, q);
        const float m = rbf(mean), var = rbf(q * (1.f / 512.f));
        const float den = rbf(sqrtf(rbf(var + 1e-5f)));
        u32x4 o;
#pragma unroll
        for (int k = 0; k < 4; ++k) o[k] = pk(rbf(v[2 * k] - m) / den, rbf(v[2 * k + 1] - m) / den);
        *(u32x4*)&L.xnb[hw][lane * 4] = o;
        if (hw == 0 && lane == 0) { L.tag[0] = tA; L.tag[1] = tB; }
    }
    STAMP(13);
    larrive(&L.cnt[C_XN]);
    lwait(&L.cnt[C_XN], 4);
    STAMP(1);
    {
        float acc[4][8];
        u32x2 a[4];
#pragma unroll
        for (int bb = 0; bb < 4; ++bb) a[bb] = *(const u32x2*)&L.xnb[bb][hw * 64 + (lane >> 1) * 2];
#pragma unroll
        for (int e = 0; e < 8; ++e) {
            const u32 s = (e & 1) ? SEL_HI : SEL_LO;
            u32 p0 = prm(wqr[1][e >> 1], wqr[0][e >> 1], s), p1 = prm(wqr[3][e >> 1], wqr[2][e >> 1], s);
#pragma unroll
            for (int bb = 0; bb < 4; ++bb) acc[bb][e] = dot2(p1, a[bb][1], dot2(p0, a[bb][0], 0.f));
        }
        f32x4 o0, o1;
#pragma unroll
        for (int e = 0; e < 8; ++e) {
            float r = sw16(sw32(acc[0][e], acc[2][e]), sw32(acc[1][e], acc[3][e]));
            r += dpp<ROR8>(r); r += dpp<ROR4>(r); r += dpp<ROR2>(r);
            if (e < 4) o0[e] = r; else o1[e - 4] = r;
        }
        if (r16 < 2) {
            *(f32x4*)&L.qpart[hw][g4][r16 * 8] = o0;
            *(f32x4*)&L.qpart[hw][g4][r16 * 8 + 4] = o1;
        }
    }
    STAMP(14);
    larrive(&L.cnt[C_QP]);
    if (hw == 0) {
        lwait(&L.cnt[C_QP], 4);
        if (lane < 16) {
            const int bb = lane >> 2, gq = lane & 3;
            f32x4 s = *(f32x4*)&L.qpart[0][bb][gq * 4];
#pragma unroll
            for (int k = 1; k < 4; ++k) s += *(f32x4*)&L.qpart[k][bb][gq * 4];
            gput(rws, WS_QX + ((hkv * 4 + bb) * 128 + j * 4 + gq) * 16, pk(s[0], s[1]), pk(s[2], s[3]), tA, tB);
        }
        STAMP(2);
    }
    u32x4 w2a, w2b;
    u32 b1p = 0;
    if (mlp) {
        u32 a[4];
        float o[8];
#pragma unroll
        for (int bb = 0; bb < 4; ++bb) a[bb] = L.xnb[bb][ks * 32 + hw * 8 + (lane >> 3)];
        tile_mac(w1a, w1b, a, o);
        if (!(lane & 8)) {
            *(f32x4*)&L.part1[hw][g4][(lane & 7) * 8] = (f32x4){o[0], o[1], o[2], o[3]};
            *(f32x4*)&L.part1[hw][g4][(lane & 7) * 8 + 4] = (f32x4){o[4], o[5], o[6], o[7]};
        }
        larrive(&L.cnt[C_P1]);
        if (hw == 1) {
            lwait(&L.cnt[C_P1], 4);
#pragma unroll
            for (int k = 0; k < 2; ++k) {
                const int idx = k * 64 + lane, bb = idx >> 5, p = idx & 31;
                float s0 = 0.f, s1 = 0.f;
#pragma unroll
                for (int q = 0; q < 4; ++q) { s0 += L.part1[q][bb][2 * p]; s1 += L.part1[q][bb][2 * p + 1]; }
                gput(rws, WS_H1 + ((fs * 8 + ks) * 128 + idx) * 16, asu(s0), asu(s1), tA, tB);
            }
        }
    }
    if (hw == 0) {
        until<2>(t0 + US16(QPOLL));
        u32x4 g[2];
        const int vo[2] = {WS_QX + ((hkv * 4 + b) * 128 + lane) * 16, WS_QX + ((hkv * 4 + b) * 128 + 64 + lane) * 16};
        gsweep<2, 4>(g, rws, vo, tA, tB);
        *(u32x2*)&L.qs[lane * 2] = (u32x2){g[0][0], g[0][2]};
        *(u32x2*)&L.qs[128 + lane * 2] = (u32x2){g[1][0], g[1][2]};
        LGKM0();
        if (lane == 0) __hip_atomic_store(&L.cnt[F_Q], 1u, __ATOMIC_RELAXED, __HIP_MEMORY_SCOPE_WORKGROUP);
        STAMP(3);
    }
    if (mlp) {
        lwait<8>(&L.cnt[F_Q], 1);
        until<8>(t0 + US16(HPOLL));
        {
            const rsrc_t r2 = mkrs(w2, 1792 * 512 * 2);
            const int vo = (fs * 64 + hw * 16 + (lane >> 3) * 2) * 1024 + (ks * 64 + (lane & 7) * 8) * 2;
            w2a = ldw(r2, vo, 0);
            w2b = ldw(r2, vo, 1024);
        }
        b1p = *(const u32*)(b1 + fs * 64 + (lane & 31) * 2);
        u32 a[4];
        float o[8];
        {
            u32x4 g[4];
            int vo[4];
#pragma unroll
            for (int k = 0; k < 4; ++k) vo[k] = WS_H1 + ((fs * 8 + 2 * k + (lane >> 5)) * 128 + hw * 32 + (lane & 31)) * 16;
            gsweep<4, 32>(g, rws, vo, tA, tB);
            float s0 = (asf(g[0][0]) + asf(g[1][0])) + (asf(g[2][0]) + asf(g[3][0]));
            float s1 = (asf(g[0][2]) + asf(g[1][2])) + (asf(g[2][2]) + asf(g[3][2]));
            s0 = sw32(s0, s0); s1 = sw32(s1, s1);
            float h0 = fmaxf(rbf(rbf(s0) + blo(b1p)), 0.f), h1 = fmaxf(rbf(rbf(s1) + bhi(b1p)), 0.f);
            if (lane < 32) L.hb[hw][lane] = pk(h0 * h0, h1 * h1);
        }
        larrive(&L.cnt[C_H]);
        lwait<2>(&L.cnt[C_H], 4);
#pragma unroll
        for (int bb = 0; bb < 4; ++bb) a[bb] = L.hb[bb][hw * 8 + (lane >> 3)];
        tile_mac(w2a, w2b, a, o);
        if (!(lane & 8)) {
            *(f32x4*)&L.part2[hw][g4][(lane & 7) * 8] = (f32x4){o[0], o[1], o[2], o[3]};
            *(f32x4*)&L.part2[hw][g4][(lane & 7) * 8 + 4] = (f32x4){o[4], o[5], o[6], o[7]};
        }
        larrive(&L.cnt[C_P2]);
        if (hw == 1) {
            lwait<2>(&L.cnt[C_P2], 4);
#pragma unroll
            for (int k = 0; k < 2; ++k) {
                const int idx = k * 64 + lane, bb = idx >> 5, p = idx & 31;
                float s0 = 0.f, s1 = 0.f;
#pragma unroll
                for (int q = 0; q < 4; ++q) { s0 += L.part2[q][bb][2 * p]; s1 += L.part2[q][bb][2 * p + 1]; }
                gput(rws, WS_G2 + ((bb * 64 + ks * 8 + (p >> 2)) * 32 + fs) * 64 + (p & 3) * 16, asu(s0), asu(s1), tA, tB);
            }
        }
    }
    STAMP(4);
    u32 W[8][8];
    {
        const rsrc_t ro = mkrs(wo, 4096 * 512 * 2);
        const int vo = (hkv * 512 + sp * 64 + 16 * g4) * 1024 + (128 * hw + 8 * r16) * 2;
        u32x4 t[16];
        u32 tw = t0 + US16(WPOLL) + hw * US16(0.25);
#pragma unroll
        for (int i = 0; i < 16; ++i) {
            if ((i & 3) == 0) { __builtin_amdgcn_sched_barrier(0); until<8>(tw); tw += US16(1.0); __builtin_amdgcn_sched_barrier(0); }
            t[i] = ldo(ro, vo + (i & 3) * 1024, (i >> 2) * 4096);
        }
#pragma unroll
        for (int p = 0; p < 8; ++p)
#pragma unroll
            for (int e = 0; e < 8; ++e) W[p][e] = prm(t[2 * p + 1][e >> 1], t[2 * p][e >> 1], (e & 1) ? SEL_HI : SEL_LO);
    }
    STAMP(15);
    if (hw == 0) {
        lwait<8>(&L.cnt[C_ML], 4);
        const int h = lane & 3, wv = (lane >> 2) & 3;
        const float mw = L.ml[wv][h][0], lw = L.ml[wv][h][1];
        float ms = fmaxf(mw, dpp<ROR4>(mw));
        ms = fmaxf(ms, dpp<ROR8>(ms));
        float ls = lw * __expf(mw - ms);
        ls += dpp<ROR4>(ls);
        ls += dpp<ROR8>(ls);
        if (lane < 4) gput(rws, WS_ML + ((grp * 8 + sp) * 4 + lane) * 16, asu(ms), asu(ls), tA, tB);
        until<16>(now16() + US16(3.0));
        u32x4 g[1];
        const int vo[1] = {WS_ML + (grp * 32 + (lane & 31)) * 16};
        gsweep<1, 32>(g, rws, vo, tA, tB);
        const float m2 = asf(g[0][0]), l2 = asf(g[0][2]);
        float M = fmaxf(m2, dpp<ROR4>(m2));
        M = fmaxf(M, dpp<ROR8>(M));
        M = mx16(M);
        float Ls = l2 * __expf(m2 - M);
        Ls += dpp<ROR4>(Ls);
        Ls += dpp<ROR8>(Ls);
        Ls = sw16(Ls, Ls);
        if (lane < 16) L.alpha[wv][h] = __expf(mw - M) / Ls;
        LGKM0();
        if (lane == 0) __hip_atomic_store(&L.cnt[F_AL], 1u, __ATOMIC_RELAXED, __HIP_MEMORY_SCOPE_WORKGROUP);
    }
    STAMP(6);
    const int rb = blk >> 6, rcb = blk & 63;
    float gate0 = 0.f, gate1 = 0.f;
    u32 xp = 0;
    if (hw == 3) {
        const u32 b2p = *(const u32*)(b2 + rcb * 8 + (lane & 3) * 2);
        xp = *(const u32*)(x + rb * 512 + rcb * 8 + (lane & 3) * 2);
        lwait<16>(&L.cnt[C_ML], 4);
        u32x4 g[2];
        const int vo[2] = {WS_G2 + blk * 2048 + lane * 16, WS_G2 + blk * 2048 + (lane < 48 ? 1024 : 0) + lane * 16};
        gsweep<2, 32>(g, rws, vo, tA, tB);
        float s0 = asf(g[0][0]) + (lane < 48 ? asf(g[1][0]) : 0.f), s1 = asf(g[0][2]) + (lane < 48 ? asf(g[1][2]) : 0.f);
        s0 += dpp<ROR4>(s0); s1 += dpp<ROR4>(s1);
        s0 += dpp<ROR8>(s0); s1 += dpp<ROR8>(s1);
        s0 = sw16(s0, s0); s1 = sw16(s1, s1);
        s0 = sw32(s0, s0); s1 = sw32(s1, s1);
        float u0 = rbf(rbf(s0) + blo(b2p)), u1 = rbf(rbf(s1) + bhi(b2p));
        gate0 = rbf(u0 / (1.f + expf(-u0)));
        gate1 = rbf(u1 / (1.f + expf(-u1)));
    }
    STAMP(5);
    lwait<1>(&L.cnt[F_SD], 1);
    STAMP(7);
    {
        u32x4 g[4];
        int vo[4];
#pragma unroll
        for (int k = 0; k < 4; ++k) vo[k] = WS_OA + (grp * 8 + sp) * 4096 + k * 1024 + lane * 16;
        gsweep<4, 1>(g, rws, vo, tA, tB);
        STAMP(8);
        float s0 = (asf(g[0][0]) + asf(g[1][0])) + (asf(g[2][0]) + asf(g[3][0]));
        float s1 = (asf(g[0][2]) + asf(g[1][2])) + (asf(g[2][2]) + asf(g[3][2]));
        s0 = sw32(s0, s0); s1 = sw32(s1, s1);
        const int o2 = (int)pk(s0, s1);
        float acc[8];
#pragma unroll
        for (int e = 0; e < 8; ++e) acc[e] = 0.f;
#pragma unroll
        for (int p = 0; p < 8; ++p) {
            const u32 op = (u32)__builtin_amdgcn_ds_bpermute(((p & 3) * 8 + 2 * g4 + (p >> 2)) * 4, o2);
#pragma unroll
            for (int e = 0; e < 8; ++e) acc[e] = dot2(W[p][e], op, acc[e]);
        }
        float r[4];
#pragma unroll
        for (int e = 0; e < 4; ++e) r[e] = sw32(acc[e], acc[e + 4]);
        const float c0 = sw16(r[0], r[2]), c1 = sw16(r[1], r[3]);
        gput(rws, WS_SB + ((b * 64 + 16 * hw + r16) * 64 + hkv * 8 + sp) * 64 + g4 * 16, asu(c0), asu(c1), tA, tB);
    }
    STAMP(9);
    if (hw == 3) {
        u32x4 g[4];
        int vo[4];
#pragma unroll
        for (int k = 0; k < 4; ++k) vo[k] = WS_SB + blk * 4096 + k * 1024 + lane * 16;
        gsweep<4, 1>(g, rws, vo, tA, tB);
        float s0 = (asf(g[0][0]) + asf(g[1][0])) + (asf(g[2][0]) + asf(g[3][0]));
        float s1 = (asf(g[0][2]) + asf(g[1][2])) + (asf(g[2][2]) + asf(g[3][2]));
        s0 += dpp<ROR4>(s0); s1 += dpp<ROR4>(s1);
        s0 += dpp<ROR8>(s0); s1 += dpp<ROR8>(s1);
        s0 = sw16(s0, s0); s1 = sw16(s1, s1);
        s0 = sw32(s0, s0); s1 = sw32(s1, s1);
        const u32 o = pk(blo(xp) + rbf(rbf(s0) * gate0), bhi(xp) + rbf(rbf(s1) * gate1));
        if (lane < 4) *(u32*)(out + rb * 512 + rcb * 8 + lane * 2) = o;
        if (blk == 0 && lane == 0) __hip_atomic_store((u32*)(ws + WS_EPOCH), tA, __ATOMIC_RELAXED, __HIP_MEMORY_SCOPE_AGENT);
        STAMP(10);
    }
    STAMPDUMP();
}

extern "C" void kernel_launch(void* const* d_in, const int* in_sizes, int n_in, void* d_out, int out_size, void* d_ws,
                              size_t ws_size, hipStream_t stream) {
    fused_decode<<<256, 512, 0, stream>>>((const uint16_t*)d_in[0], (const uint16_t*)d_in[1], (const uint16_t*)d_in[2],
                                          (const uint16_t*)d_in[3], (const uint16_t*)d_in[4], (const uint16_t*)d_in[6],
                                          (const uint16_t*)d_in[7], (const uint16_t*)d_in[8], (const uint16_t*)d_in[9],
                                          (uint16_t*)d_out, (char*)d_ws);
}
```

```cpp
#define AUXW 0
#define AUXO 0
#define MFDOT
#define ALH
#define NOREBASE
#define TL
#define TLC 2
#define PACE 970
#define ILV 8
#define ROT grp
#define W1LATE 12
#define HPOLL 16
#define WPOLL 19
#include <hip/hip_runtime.h>
#include <stdint.h>

typedef uint32_t u32;
typedef uint32_t u32x2 __attribute__((ext_vector_type(2)));
typedef uint32_t u32x4 __attribute__((ext_vector_type(4)));
typedef float f32x4 __attribute__((ext_vector_type(4)));
typedef __bf16 bf16x8 __attribute__((ext_vector_type(8)));
typedef __bf16 bf16x2 __attribute__((ext_vector_type(2)));
typedef __amdgpu_buffer_rsrc_t rsrc_t;

#define RING 32
#define SPIN_MAX (1 << 15)

#define WS_EPOCH 0
#define WS_QX 4096
#define WS_H1 (WS_QX + 65536)
#define WS_G2 (WS_H1 + 28 * 8 * 2048)
#define WS_ML (WS_G2 + 256 * 2048)
#define WS_OA (WS_ML + 32 * 8 * 64)
#define WS_SB (WS_OA + 256 * 4096)
#define WS_RD (WS_SB + 256 * 4096)
#define WS_END (WS_RD + 4096)
#define EMPTY 0xffffffffu
#define WS_STAMP (8u << 20)

enum { C_XN, C_QP, F_Q, C_P1, C_H, C_P2, C_ML, F_AL, C_O, F_SD, C_CLR, C_N = 16 };

struct __attribute__((aligned(16))) Lds {
    u32 cnt[C_N];
    u32 xnb[4][256];
    float qpart[4][4][16];
    u32 qs[256];
    float part1[4][4][64];
    float part2[4][4][64];
    u32 hb[4][32];
    float ml[4][4][2];
    float alpha[4][4];
    float ored[4][512];
    u32 tag[4];
    u32 o2[32];
};

typedef __attribute__((address_space(3))) void* ldsp;
static __device__ __forceinline__ float asf(u32 v) { return __uint_as_float(v); }
static __device__ __forceinline__ u32 asu(float v) { return __float_as_uint(v); }
static __device__ __forceinline__ float blo(u32 v) { return asf(v << 16); }
static __device__ __forceinline__ float bhi(u32 v) { return asf(v & 0xffff0000u); }
static __device__ __forceinline__ u32 pk(float lo, float hi) {
    bf16x2 r = {(__bf16)lo, (__bf16)hi};
    return __builtin_bit_cast(u32, r);
}
static __device__ __forceinline__ float rbf(float v) { return blo(pk(v, v)); }
template <int CTRL> static __device__ __forceinline__ float dpp(float v) {
    return asf(__builtin_amdgcn_update_dpp(0u, asu(v), CTRL, 0xf, 0xf, true));
}
#define ROR8 0x128
#define ROR4 0x124
#define ROR2 0x122
#define ROR1 0x121
static __device__ __forceinline__ float sw32(float a, float b) {
    auto r = __builtin_amdgcn_permlane32_swap(asu(a), asu(b), false, false);
    return asf(r[0]) + asf(r[1]);
}
static __device__ __forceinline__ float sw16(float a, float b) {
    auto r = __builtin_amdgcn_permlane16_swap(asu(a), asu(b), false, false);
    return asf(r[0]) + asf(r[1]);
}
static __device__ __forceinline__ float mx32(float a) {
    auto r = __builtin_amdgcn_permlane32_swap(asu(a), asu(a), false, false);
    return fmaxf(asf(r[0]), asf(r[1]));
}
static __device__ __forceinline__ float mx16(float a) {
    auto r = __builtin_amdgcn_permlane16_swap(asu(a), asu(a), false, false);
    return fmaxf(asf(r[0]), asf(r[1]));
}
static __device__ __forceinline__ float dot2(u32 a, u32 b, float c) {
    return __builtin_amdgcn_fdot2_f32_bf16(__builtin_bit_cast(bf16x2, a), __builtin_bit_cast(bf16x2, b), c, false);
}
static __device__ __forceinline__ u32 prm(u32 hi, u32 lo, u32 sel) { return __builtin_amdgcn_perm(hi, lo, sel); }
#define SEL_LO 0x05040100u
#define SEL_HI 0x07060302u
static __device__ __forceinline__ f32x4 mfma(u32x4 a, u32x4 b, f32x4 c) {
    return __builtin_amdgcn_mfma_f32_16x16x32_bf16(__builtin_bit_cast(bf16x8, a), __builtin_bit_cast(bf16x8, b), c, 0, 0, 0);
}
static __device__ __forceinline__ rsrc_t mkrs(const void* p, u32 bytes) {
    return __builtin_amdgcn_make_buffer_rsrc((void*)p, 0, bytes, 0x00020000);
}
static __device__ __forceinline__ u32x4 ld16(rsrc_t r, int vo, int so) { return __builtin_amdgcn_raw_buffer_load_b128(r, vo, so, 0); }
#define AUXS 2
#define AUXN "nt"
static __device__ __forceinline__ u32x4 ldnt(rsrc_t r, int vo, int so) { return __builtin_amdgcn_raw_buffer_load_b128(r, vo, so, AUXS); }
#define AUXL 16
#define AUXP 16
static __device__ __forceinline__ u32x4 ldw(rsrc_t r, int vo, int so) { return __builtin_amdgcn_raw_buffer_load_b128(r, vo, so, AUXW); }
static __device__ __forceinline__ u32x4 ldo(rsrc_t r, int vo, int so) { return __builtin_amdgcn_raw_buffer_load_b128(r, vo, so, AUXO); }
static __device__ __forceinline__ u32x4 ldsc(rsrc_t r, int vo) { return __builtin_amdgcn_raw_buffer_load_b128(r, vo, 0, AUXL); }
static __device__ __forceinline__ void gput(rsrc_t r, int vo, u32 v0, u32 v1, u32 tA, u32 tB) {
    u32x4 g = {v0, tA, v1, tB};
    __builtin_amdgcn_raw_buffer_store_b128(g, r, vo, 0, AUXP);
}
#define LGKM0() asm volatile("s_waitcnt lgkmcnt(0)" ::: "memory")
#define CFENCE() asm volatile("" ::: "memory")
static __device__ __forceinline__ void larrive(u32* c) {
    LGKM0();
    if ((threadIdx.x & 63) == 0) __hip_atomic_fetch_add(c, 1u, __ATOMIC_RELAXED, __HIP_MEMORY_SCOPE_WORKGROUP);
    CFENCE();
}
template <int SLEEP = 1>
static __device__ __forceinline__ void lwait(u32* c, u32 target) {
#pragma unroll 1
    for (int it = 0; it < SPIN_MAX; ++it) {
        u32 v = __builtin_amdgcn_readfirstlane(__hip_atomic_load(c, __ATOMIC_RELAXED, __HIP_MEMORY_SCOPE_WORKGROUP));
        if (v >= target) break;
        __builtin_amdgcn_s_sleep(SLEEP);
    }
    CFENCE();
}
#pragma clang diagnostic ignored "-Winline-asm"
static __device__ __forceinline__ void dma16(u32 ldsaddr, int vo, u32x4 rs, int so) {
    asm volatile("s_mov_b32 m0, %0\n\ts_nop 0\n\tbuffer_load_dwordx4 %1, %2, %3 offen " AUXN " lds" ::"s"(ldsaddr), "v"(vo), "s"(rs), "s"(so) : "m0");
}
static __device__ __forceinline__ u32 now16() { return (u32)__builtin_amdgcn_s_memrealtime() << 4; }
template <int SLEEP = 1>
static __device__ __forceinline__ void until(u32 t) {
#pragma unroll 1
    for (int it = 0; it < 4096; ++it) {
        if ((int)(now16() - t) >= 0) break;
        __builtin_amdgcn_s_sleep(SLEEP);
    }
}
static __device__ __forceinline__ void pace(u32& tn, u32 step) {
    until<1>(tn);
    tn += step;
}
#define TS (ILV * 16384)
#define KVREC (31 * TS + 16384)
#define TOFF(t) ((((t) + rot) & 31) * TS)
#define US16(x) ((u32)((x) * 1600))
#define PRE 0
#define PA PACE
#define PB PA
#define PC PB
#define PD PACE
#define PE PACE
#define EOFF 0
#define WSTEP 1.0
#define QPOLL 4.0
template <int N, int SLEEP>
static __device__ __forceinline__ void gsweep(u32x4 (&g)[N], rsrc_t r, const int (&vo)[N], u32 tA, u32 tB) {
#pragma unroll 1
    for (int it = 0; it < SPIN_MAX / 8; ++it) {
        CFENCE();
        bool ok = true;
#pragma unroll
        for (int k = 0; k < N; ++k) g[k] = ldsc(r, vo[k]);
#pragma unroll
        for (int k = 0; k < N; ++k) ok = ok && g[k][1] == tA && g[k][3] == tB;
        if (__all(ok)) break;
        __builtin_amdgcn_s_sleep(SLEEP);
    }
    CFENCE();
}

static __device__ __forceinline__ u32 noe(float v) { u32 u = asu(v); return u < EMPTY ? u : EMPTY - 1u; }
template <int N, int SLEEP>
static __device__ __forceinline__ void usweep(u32x4 (&g)[N], rsrc_t r, const int (&vo)[N]) {
#pragma unroll 1
    for (int it = 0; it < SPIN_MAX / 8; ++it) {
        CFENCE();
        u32 m = 0;
#pragma unroll
        for (int k = 0; k < N; ++k) g[k] = ldsc(r, vo[k]);
#pragma unroll
        for (int k = 0; k < N; ++k) m = max(max(m, max(g[k][0], g[k][1])), max(g[k][2], g[k][3]));
        if (__all(m != EMPTY)) break;
        __builtin_amdgcn_s_sleep(SLEEP);
    }
    CFENCE();
}

#define PDEP 4
#define PSL 3
#define PSTR2(x) #x
#define PSTR(x) PSTR2(x)
#define PISSUE(first, m0, m1, m2, m3) \
    asm volatile("s_mov_b64 exec, %8\n\tbuffer_load_dwordx4 %0, %4, %12, 0 offen sc1\n\t" \
                 "s_mov_b64 exec, %9\n\tbuffer_load_dwordx4 %1, %5, %12, 0 offen sc1\n\t" \
                 "s_mov_b64 exec, %10\n\tbuffer_load_dwordx4 %2, %6, %12, 0 offen sc1\n\t" \
                 "s_mov_b64 exec, %11\n\tbuffer_load_dwordx4 %3, %7, %12, 0 offen sc1\n\t" \
                 "s_mov_b64 exec, -1" \
                 : "+v"(g[0]), "+v"(g[1]), "+v"(g[2]), "+v"(g[3]) \
                 : "v"(vo[0]), "v"(vo[1]), "v"(vo[2]), "v"(vo[3]), "s"(m0), "s"(m1), "s"(m2), "s"(m3), "s"(rs) : "memory")
static __device__ __forceinline__ void gpoll4(u32x4 (&g)[4], const u32x4 rs, const int (&vo)[4], u32 tA, u32 tB) {
    const uint64_t all = ~0ull;
#pragma unroll
    for (int k = 0; k < 4; ++k) g[k] = (u32x4){0u, ~tA, 0u, ~tB};
    asm volatile("s_nop 4" ::: "memory");
#pragma unroll
    for (int d = 0; d < PDEP; ++d) {
        PISSUE(0, all, all, all, all);
        if (d + 1 < PDEP) __builtin_amdgcn_s_sleep(PSL);
    }
#pragma unroll 1
    for (int it = 0; it < SPIN_MAX; ++it) {
        asm volatile("s_waitcnt vmcnt(%4)" : "+v"(g[0]), "+v"(g[1]), "+v"(g[2]), "+v"(g[3]) : "n"(4 * (PDEP - 1)) : "memory");
#define PEND(k) (__builtin_amdgcn_uicmp(g[k][1], tA, 33) | __builtin_amdgcn_uicmp(g[k][3], tB, 33))
        const uint64_t p0 = PEND(0), p1 = PEND(1), p2 = PEND(2), p3 = PEND(3);
        if (!((p0 | p1) | (p2 | p3))) break;
        const uint64_t m0 = p0 | 1, m1 = p1 | 1, m2 = p2 | 1, m3 = p3 | 1;
        PISSUE(0, m0, m1, m2, m3);
    }
    asm volatile("s_waitcnt vmcnt(%4)" : "+v"(g[0]), "+v"(g[1]), "+v"(g[2]), "+v"(g[3]) : "n"(4 * (PDEP - 2)) : "memory");
}
#define PKEEP(g, n) asm volatile("s_waitcnt vmcnt(" #n ")" ::"v"(g[0]), "v"(g[1]), "v"(g[2]), "v"(g[3]) : "memory")
static __device__ __forceinline__ void tile_mac(u32x4 wa, u32x4 wb, const u32 (&a)[4], float (&o)[8]) {
    float acc[4][8];
#pragma unroll
    for (int e = 0; e < 8; ++e) {
        u32 wp = prm(wb[e >> 1], wa[e >> 1], (e & 1) ? SEL_HI : SEL_LO);
#pragma unroll
        for (int b = 0; b < 4; ++b) acc[b][e] = dot2(wp, a[b], 0.f);
    }
#pragma unroll
    for (int e = 0; e < 8; ++e) {
        float r0 = sw32(acc[0][e], acc[2][e]), r1 = sw32(acc[1][e], acc[3][e]);
        float r = sw16(r0, r1);
        o[e] = r + dpp<ROR8>(r);
    }
}

#define STAMP(i) do { } while (0)
#define STAMPDUMP() do { } while (0)
#define VMW(n) asm volatile("s_waitcnt vmcnt(" #n ")" ::: "memory")

__global__ __launch_bounds__(512, 2) void fused_decode(
    const uint16_t* __restrict__ x, const uint16_t* __restrict__ kc, const uint16_t* __restrict__ vc,
    const uint16_t* __restrict__ wq, const uint16_t* __restrict__ wo, const uint16_t* __restrict__ w1,
    const uint16_t* __restrict__ b1, const uint16_t* __restrict__ w2, const uint16_t* __restrict__ b2,
    uint16_t* __restrict__ out, char* __restrict__ ws) {
    __shared__ Lds L;
    __shared__ u32x4 KB[4][32][64];
    const int lane = threadIdx.x & 63;
    const int wave = __builtin_amdgcn_readfirstlane(threadIdx.x >> 6);
    const int blk = blockIdx.x;
    const int hkv = blk & 7, j = blk >> 3, b = j >> 3, sp = j & 7;
    const int grp = b * 8 + hkv;
    const int r16 = lane & 15, g4 = lane >> 4;
    const size_t kvoff = ((size_t)grp * 16384 + (sp / ILV) * (ILV * 2048) + (sp % ILV) * 64) * 128;
    const int rot = ROT;
    const int w = wave & 3;
    const rsrc_t rk = mkrs(kc + kvoff, KVREC);
    const int tau = r16 >> 1;
    const int vok = w * 4096 + (4 * (tau & 1) + (tau >> 1)) * 256 + (r16 & 1) * 64 + g4 * 16;
    const int vov = w * 4096 + lane * 16;
#define KO1 128
#define KO2 2048
#define KO3 2176
    u32x4 ring[32];
#define EARLY 0
    if (wave == 0) {
        if (lane < C_N) L.cnt[lane] = 0;
        LGKM0();
    }
    __builtin_amdgcn_s_barrier();
    asm volatile("" ::"s"(x), "s"(kc), "s"(vc), "s"(wq), "s"(wo), "s"(w1), "s"(b1), "s"(w2), "s"(b2), "s"(out), "s"(ws));
    STAMP(16);
    const u32 t0 = now16();
    const rsrc_t rws = mkrs(ws, WS_END);
    const u32x4 rwsq = {(u32)(uint64_t)ws, (u32)((uint64_t)ws >> 32) & 0xffffu, (u32)WS_END, 0x00020000u};

    if (wave >= 4) {
        u32 tn = t0 - PRE * PACE;
#pragma unroll
        for (int n = 4 * EARLY; n < 32; ++n) {
            if ((n & 3) == 0) { __builtin_amdgcn_sched_barrier(0); pace(tn, PA); __builtin_amdgcn_sched_barrier(0); }
            ring[n] = ldnt(rk, vok + ((n & 3) == 0 ? 0 : (n & 3) == 1 ? KO1 : (n & 3) == 2 ? KO2 : KO3), TOFF(n >> 2));
        }
        __builtin_amdgcn_sched_barrier(0);
        STAMP(17);
        const u32 kb = (u32)(uintptr_t)(ldsp)&KB[w][0][0];
        {
            const uint64_t pa = (uint64_t)(kc + kvoff);
            const u32x4 rs = {(u32)pa, (u32)(pa >> 32) & 0xffffu, (u32)KVREC, 0x00020000u};
#pragma unroll 1
            for (int i = 0; i < 8; ++i) {
                pace(tn, PB);
                const u32 la = kb + i * 4096;
                const int so = TOFF(8 + i);
                dma16(la, vok, rs, so);
                dma16(la + 1024, vok + KO1, rs, so);
                dma16(la + 2048, vok + KO2, rs, so);
                dma16(la + 3072, vok + KO3, rs, so);
            }
        }
        __builtin_amdgcn_sched_barrier(0);
        STAMP(5);
        lwait(&L.cnt[F_Q], 1);
        STAMP(1);
        u32x4 F[2][2];
        {
            const int sq = r16 >> 3, uq = (r16 >> 2) & 1, h = r16 & 3;
#pragma unroll
            for (int half = 0; half < 2; ++half) {
                u32x4 q = *(const u32x4*)&L.qs[(h * 128 + 64 * half + 32 * uq + 8 * g4) / 2];
#pragma unroll
                for (int s = 0; s < 2; ++s) {
                    u32x4 z = {0, 0, 0, 0};
                    F[s][half] = (sq == s) ? q : z;
                }
            }
        }
        const bool uq1 = (r16 & 4) != 0;
        u32 R[12], T[4];
#pragma unroll
        for (int k = 0; k < 12; ++k) R[k] = 0;
#pragma unroll
        for (int k = 0; k < 4; ++k) T[k] = 0;
        float mrun = -INFINITY;
#define SCORE(a0, a1, a2, a3, sc) do { \
            f32x4 d = {0.f, 0.f, 0.f, 0.f}; \
            d = mfma(a0, F[0][0], d); d = mfma(a1, F[0][1], d); d = mfma(a2, F[1][0], d); d = mfma(a3, F[1][1], d); \
            asm volatile("" : "+v"(d)); \
            dd = d; } while (0)
#define SCFIN(sc) do { \
            u32 t_ = __builtin_amdgcn_update_dpp(0u, asu(dd[1]), 0x104, 0xf, 0x5, false); \
            t_ = __builtin_amdgcn_update_dpp(t_, asu(dd[2]), 0x114, 0xf, 0xa, false); \
            sc = (uq1 ? dd[3] : dd[0]) + asf(t_); } while (0)
#define SCPK(dst, sa, sb) do { \
            u32 p1 = pk(sa, sb); \
            u32 p2 = pk(blo(p1) * 0.08838834764831845f, bhi(p1) * 0.08838834764831845f); \
            dst = p2; \
            mrun = fmaxf(mrun, fmaxf(blo(p2), bhi(p2))); } while (0)
        {
            const uint16_t* pb = kc + kvoff;
            int vo0 = vok, vo1 = vok + KO1, vo2 = vok + KO2, vo3 = vok + KO3, so = 16;
#pragma unroll 1
            for (int it = 0; it < 3; ++it) {
                if (it == 2) { pb = vc + kvoff; vo0 = vov; vo1 = vov + 1024; vo2 = vov + 2048; vo3 = vov + 3072; so = 0; }
                const rsrc_t rn = mkrs(pb, KVREC);
                const u32 pstep = it == 0 ? PC : it == 1 ? PD : PE;
                float sprev = 0.f, lprev = 0.f;
#pragma unroll
                for (int k = 0; k < 8; ++k) R[k] = R[k + 4];
#pragma unroll
                for (int i = 0; i < 8; ++i) {
                    f32x4 dd;
                    SCORE(ring[4 * i], ring[4 * i + 1], ring[4 * i + 2], ring[4 * i + 3], sc);
                    __builtin_amdgcn_sched_barrier(0);
                    pace(tn, pstep);
                    __builtin_amdgcn_sched_barrier(0);
                    ring[4 * i + 0] = ldnt(rn, vo0, TOFF(so + i));
                    ring[4 * i + 1] = ldnt(rn, vo1, TOFF(so + i));
                    ring[4 * i + 2] = ldnt(rn, vo2, TOFF(so + i));
                    ring[4 * i + 3] = ldnt(rn, vo3, TOFF(so + i));
                    __builtin_amdgcn_sched_barrier(0);
                    float sc;
                    SCFIN(sc);
                    if (i & 1) SCPK(R[8 + (i >> 1)], sprev, sc); else sprev = sc;
                    if (it == 1) {
                        u32x4 a0, a1, a2, a3;
                        asm volatile("ds_read_b128 %0, %4 offset:%5\n\tds_read_b128 %1, %4 offset:%6\n\tds_read_b128 %2, %4 offset:%7\n\t"
                                     "ds_read_b128 %3, %4 offset:%8\n\ts_waitcnt lgkmcnt(0)"
                                     : "=&v"(a0), "=&v"(a1), "=&v"(a2), "=&v"(a3)
                                     : "v"(kb + lane * 16), "n"(i * 4096), "n"(i * 4096 + 1024), "n"(i * 4096 + 2048), "n"(i * 4096 + 3072), "v"(dd[0]));
                        SCORE(a0, a1, a2, a3, lc);
                        float lc;
                        SCFIN(lc);
                        if (i & 1) SCPK(T[i >> 1], lprev, lc); else lprev = lc;
                    }
                    __builtin_amdgcn_sched_barrier(0);
                }
                so += 8;
                STAMP(8 + it);
            }
        }
        STAMP(2);
        u32 S[16];
#pragma unroll
        for (int k = 0; k < 4; ++k) { S[k] = R[k]; S[4 + k] = T[k]; S[8 + k] = R[4 + k]; S[12 + k] = R[8 + k]; }
        mrun = fmaxf(mrun, dpp<ROR4>(mrun));
        mrun = fmaxf(mrun, dpp<ROR8>(mrun));
        mrun = mx32(mx16(mrun));
        float lsum = 0.f;
#pragma unroll
        for (int k = 0; k < 16; ++k) {
            float p0 = __builtin_amdgcn_exp2f((blo(S[k]) - mrun) * 1.4426950408889634f);
            float p1 = __builtin_amdgcn_exp2f((bhi(S[k]) - mrun) * 1.4426950408889634f);
            lsum += p0 + p1;
            S[k] = pk(p0, p1);
        }
        lsum += dpp<ROR4>(lsum);
        lsum += dpp<ROR8>(lsum);
        lsum = sw16(lsum, lsum);
        lsum = sw32(lsum, lsum);
        if (lane < 4) { L.ml[w][lane][0] = mrun; L.ml[w][lane][1] = lsum; }
        larrive(&L.cnt[C_ML]);
        u32 sel[4];
        {
            const int q4 = r16 >> 2;
            const u32 s0 = (q4 & 1) ? 0x01000c0cu : 0x0c0c0100u, s1 = (q4 & 1) ? 0x03020c0cu : 0x0c0c0302u;
            sel[0] = (q4 < 2) ? s0 : 0x0c0c0c0cu; sel[1] = (q4 >= 2) ? s0 : 0x0c0c0c0cu;
            sel[2] = (q4 < 2) ? s1 : 0x0c0c0c0cu; sel[3] = (q4 >= 2) ? s1 : 0x0c0c0c0cu;
        }
        f32x4 acc[8];
#pragma unroll
        for (int e = 0; e < 8; ++e) acc[e] = (f32x4){0.f, 0.f, 0.f, 0.f};
        bool alok = false;
        float al = 0.f;
        {
            int so = 8;
#pragma unroll 1
            for (int it = 0; it < 4; ++it) {
                if (it >= 2 && !alok) {
                    alok = __builtin_amdgcn_readfirstlane(__hip_atomic_load(&L.cnt[F_AL], __ATOMIC_RELAXED, __HIP_MEMORY_SCOPE_WORKGROUP)) >= 1;
                    CFENCE();
                    al = L.alpha[w][g4];
                }
                const rsrc_t rn = mkrs(vc + kvoff, it < 3 ? KVREC : 0);
#pragma unroll
                for (int t = 0; t < 4; ++t) {
                    u32x4 A = {prm(S[t], S[t], sel[0]), prm(S[t], S[t], sel[1]), prm(S[t], S[t], sel[2]), prm(S[t], S[t], sel[3])};
#pragma unroll
                    for (int e = 0; e < 8; ++e) {
                        u32x4 B;
#pragma unroll
                        for (int p = 0; p < 4; ++p)
                            B[p] = prm(ring[8 * t + 2 * p + 1][e >> 1], ring[8 * t + 2 * p][e >> 1], (e & 1) ? SEL_HI : SEL_LO);
                        acc[e] = mfma(A, B, acc[e]);
                    }
                    asm volatile("" : "+v"(acc[0]), "+v"(acc[1]), "+v"(acc[2]), "+v"(acc[3]), "+v"(acc[4]), "+v"(acc[5]), "+v"(acc[6]), "+v"(acc[7]));
                    __builtin_amdgcn_sched_barrier(0);
                    if (it < 3) pace(tn, PACE);
                    __builtin_amdgcn_sched_barrier(0);
#pragma unroll
                    for (int k = 0; k < 4; ++k) ring[8 * t + k] = ldnt(rn, vov + k * 1024, TOFF(so + 2 * t));
                    __builtin_amdgcn_sched_barrier(0);
                    if (it < 3) pace(tn, PACE);
                    __builtin_amdgcn_sched_barrier(0);
#pragma unroll
                    for (int k = 4; k < 8; ++k) ring[8 * t + k] = ldnt(rn, vov + (k & 3) * 1024, TOFF(so + 2 * t + 1));
                    __builtin_amdgcn_sched_barrier(0);
                }
#pragma unroll
                for (int k = 0; k < 12; ++k) S[k] = S[k + 4];
                so += 8;
                STAMP(12 + it);
            }
        }
        STAMP(3);
        if (!alok) { lwait(&L.cnt[F_AL], 1); al = L.alpha[w][g4]; }
        f32x4 o0, o1;
#pragma unroll
        for (int e = 0; e < 8; ++e) {
            float v = sw16(sw32(acc[e][0], acc[e][2]), sw32(acc[e][1], acc[e][3])) * al;
            if (e < 4) o0[e] = v; else o1[e - 4] = v;
        }
        *(f32x4*)&L.ored[w][g4 * 128 + 8 * r16] = o0;
        *(f32x4*)&L.ored[w][g4 * 128 + 8 * r16 + 4] = o1;
        LGKM0();
        u32 ticket = 0;
        if (lane == 0) ticket = __hip_atomic_fetch_add(&L.cnt[C_O], 1u, __ATOMIC_RELAXED, __HIP_MEMORY_SCOPE_WORKGROUP);
        ticket = __builtin_amdgcn_readfirstlane(ticket);
        CFENCE();
        if (ticket == 3) {
            f32x4 s0 = *(f32x4*)&L.ored[0][g4 * 128 + 8 * r16], s1 = *(f32x4*)&L.ored[0][g4 * 128 + 8 * r16 + 4];
#pragma unroll
            for (int k = 1; k < 4; ++k) {
                s0 += *(f32x4*)&L.ored[k][g4 * 128 + 8 * r16];
                s1 += *(f32x4*)&L.ored[k][g4 * 128 + 8 * r16 + 4];
            }
            const int vo = WS_OA + ((grp * 8 + 2 * g4 + (r16 >> 3)) * 8 + sp) * 256 + (r16 & 7) * 16;
            __builtin_amdgcn_raw_buffer_store_b128((u32x4){noe(s0[0]), noe(s0[1]), noe(s0[2]), noe(s0[3])}, rws, vo, 0, AUXP);
            __builtin_amdgcn_raw_buffer_store_b128((u32x4){noe(s1[0]), noe(s1[1]), noe(s1[2]), noe(s1[3])}, rws, vo + 128, 0, AUXP);
            if (lane == 0) __hip_atomic_store(&L.cnt[F_SD], 1u, __ATOMIC_RELAXED, __HIP_MEMORY_SCOPE_WORKGROUP);
        }
        STAMP(4);
        STAMPDUMP();
        return;
    }

    const int hw = wave;
    const u32 epoch = __hip_atomic_load((const u32*)(ws + WS_EPOCH), __ATOMIC_RELAXED, __HIP_MEMORY_SCOPE_AGENT);
    const u32x4 xv = ld16(mkrs(x, 4096), hw * 1024 + lane * 16, 0);
    const rsrc_t rwq = mkrs(wq, 512 * 4096 * 2);
    u32x4 wqr[4];
    {
        const int vo = (hw * 128 + (lane >> 1) * 4) * 8192 + (hkv * 512 + j * 16 + (lane & 1) * 8) * 2;
#pragma unroll
        for (int i = 0; i < 4; ++i) wqr[i] = ldw(rwq, vo, i * 8192);
    }
    const bool mlp = blk < 224;
    const int fs = mlp ? (blk >> 3) : 0, ks = blk & 7;
    u32x4 w1a, w1b;
    {
        const rsrc_t r1 = mkrs(w1, 512 * 1792 * 2);
        const int vo = (ks * 64 + hw * 16 + (lane >> 3) * 2) * 3584 + (fs * 64 + (lane & 7) * 8) * 2;
    }
#define CLEAR() __builtin_amdgcn_raw_buffer_store_b128((u32x4){EMPTY, EMPTY, EMPTY, EMPTY}, rws, \
        (hw < 2 ? WS_OA + (grp * 8 + sp) * 2048 : WS_SB + blk * 2048) + (hw & 1) * 1024 + lane * 16, 0, AUXP)
    __builtin_amdgcn_sched_barrier(0);
    STAMP(17);
    const u32 tA = epoch + 1u, tB = tA ^ 0x5a5a5a5au;
    STAMP(11);
    {
        float v[8];
#pragma unroll
        for (int k = 0; k < 4; ++k) { v[2 * k] = blo(xv[k]); v[2 * k + 1] = bhi(xv[k]); }
        float s = ((v[0] + v[1]) + (v[2] + v[3])) + ((v[4] + v[5]) + (v[6] + v[7]));
        s += dpp<ROR1>(s); s += dpp<ROR2>(s); s += dpp<ROR4>(s); s += dpp<ROR8>(s);
        s = sw16(s, s); s = sw32(s, s);
        const float mean = s * (1.f / 512.f);
        float q = 0.f;
#pragma unroll
        for (int k = 0; k < 8; ++k) { float d = v[k] - mean; q = fmaf(d, d, q); }
        q += dpp<ROR1>(q); q += dpp<ROR2>(q); q += dpp<ROR4>(q); q += dpp<ROR8>(q);
        q = sw16(q, q); q = sw32(q, q);
        const float m = rbf(mean), var = rbf(q * (1.f / 512.f));
        const float den = rbf(sqrtf(rbf(var + 1e-5f)));
        u32x4 o;
#pragma unroll
        for (int k = 0; k < 4; ++k) o[k] = pk(rbf(v[2 * k] - m) / den, rbf(v[2 * k + 1] - m) / den);
        *(u32x4*)&L.xnb[hw][lane * 4] = o;
        if (hw == 0 && lane == 0) { L.tag[0] = tA; L.tag[1] = tB; }
    }
    STAMP(13);
    larrive(&L.cnt[C_XN]);
    lwait(&L.cnt[C_XN], 4);
    STAMP(1);
    {
        float acc[4][8];
        u32x2 a[4];
#pragma unroll
        for (int bb = 0; bb < 4; ++bb) a[bb] = *(const u32x2*)&L.xnb[bb][hw * 64 + (lane >> 1) * 2];
#pragma unroll
        for (int e = 0; e < 8; ++e) {
            const u32 s = (e & 1) ? SEL_HI : SEL_LO;
            u32 p0 = prm(wqr[1][e >> 1], wqr[0][e >> 1], s), p1 = prm(wqr[3][e >> 1], wqr[2][e >> 1], s);
#pragma unroll
            for (int bb = 0; bb < 4; ++bb) acc[bb][e] = dot2(p1, a[bb][1], dot2(p0, a[bb][0], 0.f));
        }
        f32x4 o0, o1;
#pragma unroll
        for (int e = 0; e < 8; ++e) {
            float r = sw16(sw32(acc[0][e], acc[2][e]), sw32(acc[1][e], acc[3][e]));
            r += dpp<ROR8>(r); r += dpp<ROR4>(r); r += dpp<ROR2>(r);
            if (e < 4) o0[e] = r; else o1[e - 4] = r;
        }
        if (r16 < 2) {
            *(f32x4*)&L.qpart[hw][g4][r16 * 8] = o0;
            *(f32x4*)&L.qpart[hw][g4][r16 * 8 + 4] = o1;
        }
    }
    STAMP(14);
    larrive(&L.cnt[C_QP]);
    if (hw == 0) {
        lwait(&L.cnt[C_QP], 4);
        if (lane < 16) {
            const int bb = lane >> 2, gq = lane & 3;
            f32x4 s = *(f32x4*)&L.qpart[0][bb][gq * 4];
#pragma unroll
            for (int k = 1; k < 4; ++k) s += *(f32x4*)&L.qpart[k][bb][gq * 4];
            gput(rws, WS_QX + ((hkv * 4 + bb) * 128 + j * 4 + gq) * 16, pk(s[0], s[1]), pk(s[2], s[3]), tA, tB);
        }
        STAMP(2);
    }
#define QXCH() do { if (hw == 0) { \
        until<2>(t0 + US16(QPOLL)); \
        u32x4 g[2]; \
        const int vo[2] = {WS_QX + ((hkv * 4 + b) * 128 + lane) * 16, WS_QX + ((hkv * 4 + b) * 128 + 64 + lane) * 16}; \
        gsweep<2, 4>(g, rws, vo, tA, tB); \
        *(u32x2*)&L.qs[lane * 2] = (u32x2){g[0][0], g[0][2]}; \
        *(u32x2*)&L.qs[128 + lane * 2] = (u32x2){g[1][0], g[1][2]}; \
        LGKM0(); \
        if (lane == 0) __hip_atomic_store(&L.cnt[F_Q], 1u, __ATOMIC_RELAXED, __HIP_MEMORY_SCOPE_WORKGROUP); \
        STAMP(3); \
    } } while (0)
    QXCH();
    u32x4 w2a, w2b;
    u32 b1p = 0;
    if (mlp) {
        u32 a[4];
        float o[8];
        until<8>(t0 + US16(W1LATE));
        {
            const rsrc_t r1 = mkrs(w1, 512 * 1792 * 2);
            const int vo = (ks * 64 + hw * 16 + (lane >> 3) * 2) * 3584 + (fs * 64 + (lane & 7) * 8) * 2;
            w1a = ldw(r1, vo, 0);
            w1b = ldw(r1, vo, 3584);
        }
#pragma unroll
        for (int bb = 0; bb < 4; ++bb) a[bb] = L.xnb[bb][ks * 32 + hw * 8 + (lane >> 3)];
        tile_mac(w1a, w1b, a, o);
        if (!(lane & 8)) {
            *(f32x4*)&L.part1[hw][g4][(lane & 7) * 8] = (f32x4){o[0], o[1], o[2], o[3]};
            *(f32x4*)&L.part1[hw][g4][(lane & 7) * 8 + 4] = (f32x4){o[4], o[5], o[6], o[7]};
        }
        larrive(&L.cnt[C_P1]);
        if (hw == 1) {
            lwait(&L.cnt[C_P1], 4);
#pragma unroll
            for (int k = 0; k < 2; ++k) {
                const int idx = k * 64 + lane, bb = idx >> 5, p = idx & 31;
                float s0 = 0.f, s1 = 0.f;
#pragma unroll
                for (int q = 0; q < 4; ++q) { s0 += L.part1[q][bb][2 * p]; s1 += L.part1[q][bb][2 * p + 1]; }
                gput(rws, WS_H1 + ((fs * 8 + ks) * 128 + idx) * 16, asu(s0), asu(s1), tA, tB);
            }
        }
    }
    if (mlp) {
        lwait<8>(&L.cnt[F_Q], 1);
        until<8>(t0 + US16(HPOLL));
        {
            const rsrc_t r2 = mkrs(w2, 1792 * 512 * 2);
            const int vo = (fs * 64 + hw * 16 + (lane >> 3) * 2) * 1024 + (ks * 64 + (lane & 7) * 8) * 2;
            w2a = ldw(r2, vo, 0);
            w2b = ldw(r2, vo, 1024);
        }
        b1p = *(const u32*)(b1 + fs * 64 + (lane & 31) * 2);
        u32 a[4];
        float o[8];
        {
            u32x4 g[4];
            int vo[4];
#pragma unroll
            for (int k = 0; k < 4; ++k) vo[k] = WS_H1 + ((fs * 8 + 2 * k + (lane >> 5)) * 128 + hw * 32 + (lane & 31)) * 16;
            gsweep<4, 32>(g, rws, vo, tA, tB);
            float s0 = (asf(g[0][0]) + asf(g[1][0])) + (asf(g[2][0]) + asf(g[3][0]));
            float s1 = (asf(g[0][2]) + asf(g[1][2])) + (asf(g[2][2]) + asf(g[3][2]));
            s0 = sw32(s0, s0); s1 = sw32(s1, s1);
            float h0 = fmaxf(rbf(rbf(s0) + blo(b1p)), 0.f), h1 = fmaxf(rbf(rbf(s1) + bhi(b1p)), 0.f);
            if (lane < 32) L.hb[hw][lane] = pk(h0 * h0, h1 * h1);
        }
        larrive(&L.cnt[C_H]);
        lwait<2>(&L.cnt[C_H], 4);
#pragma unroll
        for (int bb = 0; bb < 4; ++bb) a[bb] = L.hb[bb][hw * 8 + (lane >> 3)];
        tile_mac(w2a, w2b, a, o);
        if (!(lane & 8)) {
            *(f32x4*)&L.part2[hw][g4][(lane & 7) * 8] = (f32x4){o[0], o[1], o[2], o[3]};
            *(f32x4*)&L.part2[hw][g4][(lane & 7) * 8 + 4] = (f32x4){o[4], o[5], o[6], o[7]};
        }
        larrive(&L.cnt[C_P2]);
        if (hw == 1) {
            lwait<2>(&L.cnt[C_P2], 4);
#pragma unroll
            for (int k = 0; k < 2; ++k) {
                const int idx = k * 64 + lane, bb = idx >> 5, p = idx & 31;
                float s0 = 0.f, s1 = 0.f;
#pragma unroll
                for (int q = 0; q < 4; ++q) { s0 += L.part2[q][bb][2 * p]; s1 += L.part2[q][bb][2 * p + 1]; }
                gput(rws, WS_G2 + ((bb * 64 + ks * 8 + (p >> 2)) * 32 + fs) * 64 + (p & 3) * 16, asu(s0), asu(s1), tA, tB);
            }
        }
    }
    STAMP(4);
    u32x4 W[2][8];
    {
        const rsrc_t ro = mkrs(wo, 4096 * 512 * 2);
        const int vo = (hkv * 512 + sp * 64 + 16 * g4) * 1024 + (128 * hw + 8 * r16) * 2;
        u32x4 t[16];
        u32 tw = t0 + US16(WPOLL) + hw * US16(0.25);
#pragma unroll
        for (int i = 0; i < 16; ++i) {
            if ((i & 3) == 0) { __builtin_amdgcn_sched_barrier(0); until<8>(tw); tw += US16(WSTEP); __builtin_amdgcn_sched_barrier(0); }
            t[i] = ldo(ro, vo + (i & 3) * 1024, (i >> 2) * 4096);
        }
#pragma unroll
        for (int p = 0; p < 8; ++p)
#pragma unroll
            for (int e = 0; e < 8; ++e) W[p >> 2][e][p & 3] = prm(t[2 * p + 1][e >> 1], t[2 * p][e >> 1], (e & 1) ? SEL_HI : SEL_LO);
#pragma unroll
        for (int e = 0; e < 8; ++e) asm volatile("" : "+v"(W[0][e]), "+v"(W[1][e]));
    }
    CLEAR();
    VMW(0);
    larrive(&L.cnt[C_CLR]);
    if (hw == 1) {
        lwait<2>(&L.cnt[C_CLR], 4);
        if (lane == 0) gput(rws, WS_RD + blk * 16, 0u, 0u, tA, tB);
    }
    STAMP(15);
    if (hw == 0) {
        lwait<8>(&L.cnt[C_ML], 4);
        const int h = lane & 3, wv = (lane >> 2) & 3;
        const float mw = L.ml[wv][h][0], lw = L.ml[wv][h][1];
        float ms = fmaxf(mw, dpp<ROR4>(mw));
        ms = fmaxf(ms, dpp<ROR8>(ms));
        float ls = lw * __expf(mw - ms);
        ls += dpp<ROR4>(ls);
        ls += dpp<ROR8>(ls);
        if (lane < 4) gput(rws, WS_ML + ((grp * 8 + sp) * 4 + lane) * 16, asu(ms), asu(ls), tA, tB);
        until<16>(now16() + US16(3.0));
        u32x4 g[2];
        const int vo[2] = {WS_ML + (grp * 32 + (lane & 31)) * 16, WS_RD + (b * 64 + lane) * 16};
        gsweep<2, 32>(g, rws, vo, tA, tB);
        const float m2 = asf(g[0][0]), l2 = asf(g[0][2]);
        float M = fmaxf(m2, dpp<ROR4>(m2));
        M = fmaxf(M, dpp<ROR8>(M));
        M = mx16(M);
        float Ls = l2 * __expf(m2 - M);
        Ls += dpp<ROR4>(Ls);
        Ls += dpp<ROR8>(Ls);
        Ls = sw16(Ls, Ls);
        if (lane < 16) L.alpha[wv][h] = __expf(mw - M) / Ls;
        LGKM0();
        if (lane == 0) __hip_atomic_store(&L.cnt[F_AL], 1u, __ATOMIC_RELAXED, __HIP_MEMORY_SCOPE_WORKGROUP);
    }
    STAMP(6);
    const int rb = blk >> 6, rcb = blk & 63;
    float gate0 = 0.f, gate1 = 0.f;
    u32 xp = 0;
    if (hw == 3) {
        const u32 b2p = *(const u32*)(b2 + rcb * 8 + (lane & 3) * 2);
        xp = *(const u32*)(x + rb * 512 + rcb * 8 + (lane & 3) * 2);
        lwait<16>(&L.cnt[C_ML], 4);
        u32x4 g[2];
        const int vo[2] = {WS_G2 + blk * 2048 + lane * 16, WS_G2 + blk * 2048 + (lane < 48 ? 1024 : 0) + lane * 16};
        gsweep<2, 32>(g, rws, vo, tA, tB);
        float s0 = asf(g[0][0]) + (lane < 48 ? asf(g[1][0]) : 0.f), s1 = asf(g[0][2]) + (lane < 48 ? asf(g[1][2]) : 0.f);
        s0 += dpp<ROR4>(s0); s1 += dpp<ROR4>(s1);
        s0 += dpp<ROR8>(s0); s1 += dpp<ROR8>(s1);
        s0 = sw16(s0, s0); s1 = sw16(s1, s1);
        s0 = sw32(s0, s0); s1 = sw32(s1, s1);
        float u0 = rbf(rbf(s0) + blo(b2p)), u1 = rbf(rbf(s1) + bhi(b2p));
        gate0 = rbf(u0 / (1.f + expf(-u0)));
        gate1 = rbf(u1 / (1.f + expf(-u1)));
        const int src = (2 * (r16 & 1) + (g4 >> 1)) * 4;
        const u32 ga = (u32)__builtin_amdgcn_ds_bpermute(src, (int)asu(gate0)), gb = (u32)__builtin_amdgcn_ds_bpermute(src, (int)asu(gate1));
        const u32 xq = (u32)__builtin_amdgcn_ds_bpermute(src, (int)xp);
        gate0 = asf((g4 & 1) ? gb : ga);
        xp = (g4 & 1) ? xq & 0xffff0000u : xq << 16;
    }
    STAMP(5);
    lwait<1>(&L.cnt[F_SD], 1);
    STAMP(7);
    {
        u32x4 g[2];
        const int vo[2] = {WS_OA + (grp * 8 + sp) * 2048 + lane * 16, WS_OA + (grp * 8 + sp) * 2048 + 1024 + lane * 16};
        usweep<2, 1>(g, rws, vo);
        STAMP(8);
        float oa = sw32(asf(g[0][0]) + asf(g[1][0]), asf(g[0][2]) + asf(g[1][2]));
        float ob = sw32(asf(g[0][1]) + asf(g[1][1]), asf(g[0][3]) + asf(g[1][3]));
        oa = sw16(oa, oa); ob = sw16(ob, ob);
        const int o2 = (int)pk(oa, ob);
#define OSRC(p) ((((p) & 1) * 32 + (((p) >> 1) & 1) * 8 + 2 * g4 + ((p) >> 2)) * 4)
        u32 op[8];
#pragma unroll
        for (int p = 0; p < 8; ++p) op[p] = (u32)__builtin_amdgcn_ds_bpermute(OSRC(p), o2);
        float cc[8];
#pragma unroll
        for (int e = 0; e < 8; ++e) {
            f32x4 d = {0.f, 0.f, 0.f, 0.f};
            d = mfma((u32x4){op[0], op[1], op[2], op[3]}, W[0][e], d);
            d = mfma((u32x4){op[4], op[5], op[6], op[7]}, W[1][e], d);
            cc[e] = d[0];
        }
        const bool g1 = g4 & 1, g2 = g4 & 2;
        const float c0 = g2 ? (g1 ? cc[6] : cc[4]) : (g1 ? cc[2] : cc[0]);
        const float c1 = g2 ? (g1 ? cc[7] : cc[5]) : (g1 ? cc[3] : cc[1]);
        __builtin_amdgcn_raw_buffer_store_b64((u32x2){noe(c0), noe(c1)}, rws, WS_SB + ((b * 64 + 16 * hw + r16) * 64 + hkv * 8 + sp) * 32 + g4 * 8, 0, AUXP);
    }
    STAMP(9);
    if (hw == 3) {
        u32x4 g[2];
        const int vo[2] = {WS_SB + blk * 2048 + lane * 16, WS_SB + blk * 2048 + 1024 + lane * 16};
        usweep<2, 1>(g, rws, vo);
        float s = sw16(sw32(asf(g[0][0]) + asf(g[1][0]), asf(g[0][2]) + asf(g[1][2])),
                       sw32(asf(g[0][1]) + asf(g[1][1]), asf(g[0][3]) + asf(g[1][3])));
        s += dpp<ROR2>(s); s += dpp<ROR4>(s); s += dpp<ROR8>(s);
        const u32 o = pk(asf(xp) + rbf(rbf(s) * gate0), 0.f);
        if (r16 < 2) out[rb * 512 + rcb * 8 + 4 * r16 + g4] = (uint16_t)o;
        if (blk == 0 && lane == 0) __hip_atomic_store((u32*)(ws + WS_EPOCH), tA, __ATOMIC_RELAXED, __HIP_MEMORY_SCOPE_AGENT);
        STAMP(10);
    }
    STAMPDUMP();
}

extern "C" void kernel_launch(void* const* d_in, const int* in_sizes, int n_in, void* d_out, int out_size, void* d_ws,
                              size_t ws_size, hipStream_t stream) {
    fused_decode<<<256, 512, 0, stream>>>((const uint16_t*)d_in[0], (const uint16_t*)d_in[1], (const uint16_t*)d_in[2],
                                          (const uint16_t*)d_in[3], (const uint16_t*)d_in[4], (const uint16_t*)d_in[6],
                                          (const uint16_t*)d_in[7], (const uint16_t*)d_in[8], (const uint16_t*)d_in[9],
                                          (uint16_t*)d_out, (char*)d_ws);
}
```
